```python
import torch

def _softmax(x, axis=-1):
    return torch.softmax(x, dim=axis)
def _gelu(x):
    return torch.nn.functional.gelu(x, approximate="tanh")
def _silu(x):
    return torch.nn.functional.silu(x)
def _sigmoid(x):
    return torch.sigmoid(x)
def _layernorm(x, axis=-1, eps=1e-5):
    m = x.mean(dim=axis, keepdim=True)
    v = x.var(dim=axis, keepdim=True, unbiased=False)
    return (x - m) / torch.sqrt(v + eps)
def _scatter_add(tgt, idx, src, axis=0):
    return tgt.clone().index_add_(axis, idx, src)
def _segment_sum(src, seg, n):
    out = torch.zeros((n,) + src.shape[1:], dtype=src.dtype)
    return out.index_add_(0, seg, src)
def _pad(x, pw):
    flat = tuple(v for pair in reversed(pw) for v in pair)
    return torch.nn.functional.pad(x, flat)

kernel_name = 'gated_mix__sliding_block__spectral_fnet__prompt'

def setup_inputs():

    _w = (lambda a: (lambda t: t.to(torch.bfloat16) if t.is_floating_point() else t)(torch.as_tensor(a)))
    def _A_setup():
        B, S, D = 1, 2048, 512
        return {
            "x": _w((lambda *s: torch.randn(*s, dtype=torch.bfloat16) * 0.05)(B, S, D)),
            "wqkv": _w((lambda *s: torch.randn(*s, dtype=torch.bfloat16) * 1.6)(D, 3 * D)),
            "wo": _w((lambda *s: torch.randn(*s, dtype=torch.bfloat16) * 1.6)(D, D)),
        }
    _A_in = _A_setup()

    def _B_setup():
        B, S, D = 1, 2048, 512
        ns = torch.arange(S)[:, None] * torch.arange(S)[None, :]
        nd = torch.arange(D)[:, None] * torch.arange(D)[None, :]
        Ws_re = torch.cos(-2.0 * torch.pi * ns / S).to(float)
        Ws_im = torch.sin(-2.0 * torch.pi * ns / S).to(float)
        Wd_re = torch.cos(-2.0 * torch.pi * nd / D).to(float)
        Wd_im = torch.sin(-2.0 * torch.pi * nd / D).to(float)
        return {
            "x": _w((lambda *s: torch.randn(*s, dtype=torch.bfloat16) * 0.05)(B, S, D)),
            "dft_seq_re": _w(Ws_re),
            "dft_seq_im": _w(Ws_im),
            "dft_hid_re": _w(Wd_re),
            "dft_hid_im": _w(Wd_im),
        }
    _B_in = _B_setup()

    return {
        "A_x": _A_in["x"],
        "A_wqkv": _A_in["wqkv"],
        "A_wo": _A_in["wo"],
        "B_x": _B_in["x"],
        "B_dft_seq_re": _B_in["dft_seq_re"],
        "B_dft_seq_im": _B_in["dft_seq_im"],
        "B_dft_hid_re": _B_in["dft_hid_re"],
        "B_dft_hid_im": _B_in["dft_hid_im"],
        "gate_w": _w((lambda *s: torch.randn(*s, dtype=torch.bfloat16) * 0.05)(512) * 16.0),
    }

def reference(A_x, A_wqkv, A_wo, B_x, B_dft_seq_re, B_dft_seq_im, B_dft_hid_re, B_dft_hid_im, gate_w):
    def _A_ref(x, wqkv, wo):
        B, S, D = x.shape
        H = 32; Dh = D // H; W = 256
        qkv = (lambda x, *d: x.permute(*d))((x @ wqkv).reshape(B, S, 3, H, Dh), 2, 0, 3, 1, 4)
        q, k, v = qkv[0], qkv[1], qkv[2]
        scores = torch.einsum("bhqd,bhkd->bhqk", q, k) * (Dh ** -0.5)
        idx = torch.arange(S)
        mask = (idx[:, None] - idx[None, :] >= 0) & (idx[:, None] - idx[None, :] < W)
        scores = torch.where(mask[None, None], scores, -1e9)
        p = _softmax(scores, axis=-1)
        o = torch.einsum("bhqk,bhkd->bhqd", p, v)
        return (lambda x, *d: x.permute(*d))(o, 0, 2, 1, 3).reshape(B, S, D) @ wo

    def _B_ref(x, dft_seq_re, dft_seq_im, dft_hid_re, dft_hid_im):
        a = torch.einsum("bsd,ed->bse", x, dft_hid_re)
        b = torch.einsum("bsd,ed->bse", x, dft_hid_im)
        y = torch.einsum("ts,bse->bte", dft_seq_re, a) - torch.einsum("ts,bse->bte", dft_seq_im, b)
        return y

    x = A_x
    g = _sigmoid((x * gate_w).sum(-1, keepdims=True))
    a = _A_ref(x, A_wqkv, A_wo)
    b = _B_ref(x, B_dft_seq_re, B_dft_seq_im, B_dft_hid_re, B_dft_hid_im)
    return g * a + (1 - g) * b
```

```cpp
#include <hip/hip_runtime.h>
#include <hip/hip_bf16.h>
typedef unsigned short u16;
__device__ __forceinline__ float b2f(u16 v){ return __uint_as_float(((unsigned)v)<<16); }
__device__ __forceinline__ u16 f2b(float f){ unsigned u=__float_as_uint(f); u += 0x7fff + ((u>>16)&1); return (u16)(u>>16); }

template<int BT>
__global__ void gemm_naive(const u16* A, const u16* B, u16* C, int M, int N, int K, int ldc){
  __shared__ float sa[16][17], sb[16][17];
  int tx=threadIdx.x, ty=threadIdx.y;
  int r=blockIdx.y*16+ty, c=blockIdx.x*16+tx;
  float acc=0;
  for(int k0=0;k0<K;k0+=16){
    sa[ty][tx]=b2f(A[(long)r*K+k0+tx]);
    if(BT) sb[tx][ty]=b2f(B[(long)(blockIdx.x*16+ty)*K+k0+tx]); else sb[ty][tx]=b2f(B[(long)(k0+ty)*N+c]);
    __syncthreads();
    for(int k=0;k<16;k++) acc+=sa[ty][k]*sb[k][tx];
    __syncthreads();
  }
  C[(long)r*ldc+c]=f2b(acc);
}
__global__ void attn_naive(const u16* qkv, u16* o){
  int q=blockIdx.x, h=blockIdx.y, t=threadIdx.x;
  __shared__ float p[256]; __shared__ float red[256];
  int key=q-t; float s=-1e30f;
  if(key>=0){ s=0; for(int d=0;d<16;d++) s+=b2f(qkv[q*1536+h*16+d])*b2f(qkv[key*1536+512+h*16+d]); s*=0.25f; }
  red[t]=s; __syncthreads();
  for(int o2=128;o2>0;o2>>=1){ if(t<o2) red[t]=fmaxf(red[t],red[t+o2]); __syncthreads(); }
  float m=red[0]; __syncthreads();
  float e=(key>=0)?__expf(s-m):0.f; p[t]=e; red[t]=e; __syncthreads();
  for(int o2=128;o2>0;o2>>=1){ if(t<o2) red[t]+=red[t+o2]; __syncthreads(); }
  float l=red[0];
  if(t<16){ float acc=0; for(int j=0;j<256;j++){ int kk=q-j; if(kk>=0) acc+=p[j]*b2f(qkv[kk*1536+1024+h*16+t]); } o[q*512+h*16+t]=f2b(acc/l); }
}
__global__ void fin(const u16* x, const u16* gw, const u16* a, const u16* y1, const u16* y2, u16* out){
  int r=blockIdx.x, t=threadIdx.x; __shared__ float red[512];
  red[t]=b2f(x[r*512+t])*b2f(gw[t]); __syncthreads();
  for(int o2=256;o2>0;o2>>=1){ if(t<o2) red[t]+=red[t+o2]; __syncthreads(); }
  float g=1.f/(1.f+__expf(-red[0]));
  out[r*512+t]=f2b(g*b2f(a[r*512+t])+(1.f-g)*(b2f(y1[r*512+t])-b2f(y2[r*512+t])));
}
extern "C" void kernel_launch(void* const* d_in, const int* in_sizes, int n_in, void* d_out, int out_size, void* d_ws, size_t ws_size, hipStream_t stream){
  const u16* x=(const u16*)d_in[0]; const u16* wqkv=(const u16*)d_in[1]; const u16* wo=(const u16*)d_in[2];
  const u16* cr=(const u16*)d_in[4]; const u16* ci=(const u16*)d_in[5]; const u16* dr=(const u16*)d_in[6]; const u16* di=(const u16*)d_in[7]; const u16* gw=(const u16*)d_in[8];
  u16* ws=(u16*)d_ws; u16* qkv=ws; u16* o=qkv+2048*1536; u16* a=o+2048*512; u16* u=a+2048*512; u16* v=u+2048*512; u16* y1=v+2048*512; u16* y2=y1+2048*512;
  dim3 b(16,16);
  gemm_naive<0><<<dim3(1536/16,2048/16),b,0,stream>>>(x,wqkv,qkv,2048,1536,512,1536);
  attn_naive<<<dim3(2048,32),256,0,stream>>>(qkv,o);
  gemm_naive<0><<<dim3(512/16,2048/16),b,0,stream>>>(o,wo,a,2048,512,512,512);
  gemm_naive<0><<<dim3(512/16,2048/16),b,0,stream>>>(cr,x,u,2048,512,2048,512);
  gemm_naive<0><<<dim3(512/16,2048/16),b,0,stream>>>(ci,x,v,2048,512,2048,512);
  gemm_naive<1><<<dim3(512/16,2048/16),b,0,stream>>>(u,dr,y1,2048,512,512,512);
  gemm_naive<1><<<dim3(512/16,2048/16),b,0,stream>>>(v,di,y2,2048,512,512,512);
  fin<<<2048,512,0,stream>>>(x,gw,a,y1,y2,(u16*)d_out);
}
```

```cpp
#include <hip/hip_runtime.h>
#include <hip/hip_bf16.h>
#include <stdint.h>
#pragma clang diagnostic ignored "-Wint-to-pointer-cast"

typedef __bf16 bf16x8 __attribute__((ext_vector_type(8)));
typedef __bf16 bf16x4 __attribute__((ext_vector_type(4)));
typedef __bf16 bf16x2 __attribute__((ext_vector_type(2)));
typedef float f32x16 __attribute__((ext_vector_type(16)));
typedef float f32x2 __attribute__((ext_vector_type(2)));
typedef unsigned u32x4 __attribute__((ext_vector_type(4)));
typedef unsigned short u16;
#define AS3 __attribute__((address_space(3)))
#define DI __device__ __forceinline__
#define MFMA(a, b, c) __builtin_amdgcn_mfma_f32_32x32x16_bf16((a), (b), (c), 0, 0, 0)
#define SBAR() __builtin_amdgcn_sched_barrier(0)

constexpr int S_ = 2048, D_ = 512;
constexpr size_t ACT_BYTES = 64ull * 96 * 1024;
constexpr size_t WS_G = ACT_BYTES;
constexpr size_t WS_STAMP = 8ull << 20;

#define STAMP_DECL
#define STAMP_ARG
#define STAMP_PASS
#define STAMP(slot) do {} while (0)
#define STAMP_FLUSH(kid) do {} while (0)

DI void dma16(const void* sbase, unsigned voff, unsigned lds) {
  asm volatile("s_mov_b32 m0, %2\n\ts_nop 0\n\tglobal_load_lds_dwordx4 %0, %1" :: "v"(voff), "s"(sbase), "s"(lds) : "memory");
}
DI bf16x8 lds128(unsigned a) { return *(AS3 const bf16x8*)a; }
DI bf16x8 ldstr(unsigned a0, unsigned a1) {
  bf16x4 lo = __builtin_amdgcn_ds_read_tr16_b64_v4bf16((AS3 bf16x4*)a0);
  bf16x4 hi = __builtin_amdgcn_ds_read_tr16_b64_v4bf16((AS3 bf16x4*)a1);
  return __builtin_shufflevector(lo, hi, 0, 1, 2, 3, 4, 5, 6, 7);
}
DI unsigned pk2(float a, float b) { f32x2 p = {a, b}; return __builtin_bit_cast(unsigned, __builtin_convertvector(p, bf16x2)); }
template <int s> DI u32x4 pack8(const f32x16& x, float sc) {
  u32x4 r; r[0] = pk2(x[8 * s] * sc, x[8 * s + 1] * sc); r[1] = pk2(x[8 * s + 2] * sc, x[8 * s + 3] * sc);
  r[2] = pk2(x[8 * s + 4] * sc, x[8 * s + 5] * sc); r[3] = pk2(x[8 * s + 6] * sc, x[8 * s + 7] * sc); return r;
}
template <int s> DI u32x4 pack8(const f32x16& x) {
  u32x4 r; r[0] = pk2(x[8 * s], x[8 * s + 1]); r[1] = pk2(x[8 * s + 2], x[8 * s + 3]);
  r[2] = pk2(x[8 * s + 4], x[8 * s + 5]); r[3] = pk2(x[8 * s + 6], x[8 * s + 7]); return r;
}
DI u32x4 natural(u32x4 p) {
  auto a = __builtin_amdgcn_permlane32_swap(p[0], p[2], false, false);
  auto b = __builtin_amdgcn_permlane32_swap(p[1], p[3], false, false);
  u32x4 r; r[0] = a[0]; r[1] = b[0]; r[2] = a[1]; r[3] = b[1]; return r;
}
DI bf16x8 asbf(u32x4 v) { return __builtin_bit_cast(bf16x8, v); }
#define WAITV_BAR(N) asm volatile("s_waitcnt vmcnt(%0)\n\ts_barrier" :: "i"(N) : "memory")
#define WAITL_BAR() asm volatile("s_waitcnt lgkmcnt(0)\n\ts_barrier" ::: "memory")
#define WAITVL_BAR(N) asm volatile("s_waitcnt vmcnt(%0) lgkmcnt(0)\n\ts_barrier" :: "i"(N) : "memory")

constexpr int U_SLOT = 32768, U_NSLOT = 4, U_STEPS = 32;
DI void u_role(const u16* __restrict__ x, const u16* __restrict__ cm, char* ws, int trow0, int tn, int koff, int ksbase, bool neg, unsigned lds0 STAMP_ARG) {
  const int tid = threadIdx.x, lane = tid & 63, wid = __builtin_amdgcn_readfirstlane(tid >> 6);
  if (wid >= 4) {
    const int lw = wid - 4;
    unsigned va[4], vb[4];
#pragma unroll
    for (int i = 0; i < 4; ++i) {
      const int row = lw * 32 + i * 8 + (lane >> 3);
      va[i] = (unsigned)row * 4096u + (unsigned)(((lane & 7) ^ ((row >> 1) & 7)) * 16);
      const int kr = lw * 16 + i * 4 + (lane >> 4);
      vb[i] = (unsigned)kr * 1024u + (unsigned)(((lane & 15) ^ ((kr & 3) << 2)) * 16);
    }
    const char* abase = (const char*)(cm + (size_t)trow0 * 2048);
    const char* bbase = (const char*)(x + tn * 128);
    auto issue = [&](int s) {
      const int ks = (s + koff) & (U_STEPS - 1);
      const unsigned l = lds0 + (unsigned)(s & (U_NSLOT - 1)) * U_SLOT + lw * 4096;
      const char* ap = abase + ks * 128;
      const char* bp = bbase + (size_t)ks * 65536;
#pragma unroll
      for (int i = 0; i < 4; ++i) dma16(ap, va[i], l + i * 1024);
#pragma unroll
      for (int i = 0; i < 4; ++i) dma16(bp, vb[i], l + 16384 + i * 1024);
    };
    issue(0); issue(1); issue(2);
    for (int s = 0; s < U_STEPS - 3; ++s) { WAITV_BAR(16); issue(s + 3); }
    WAITV_BAR(16); WAITV_BAR(8); WAITV_BAR(0);
    return;
  }
  const int wm = wid & 1, wn = wid >> 1, h = lane >> 5, r32 = lane & 31;
  unsigned xa[2], ca[4];
  {
    const int blk = (lane >> 4) & 1, q = (lane & 15) >> 2, p = lane & 3;
#pragma unroll
    for (int m = 0; m < 2; ++m) xa[m] = 16384u + (unsigned)((8 * h + q) * 256 + (((8 * wm + 4 * m + 2 * blk + (p >> 1)) ^ (q << 2)) * 16) + 8 * (p & 1));
    const int row = wn * 64 + r32;
#pragma unroll
    for (int i = 0; i < 4; ++i) ca[i] = (unsigned)(row * 128 + (((2 * i + h) ^ ((row >> 1) & 7)) * 16));
  }
  f32x16 acc[2][2] = {};
  bf16x8 a0[2], b0[2], a1[2], b1[2];
#define U_LOAD(A, B, sb, i) do { \
    B[0] = lds128((sb) + ca[i]); B[1] = lds128((sb) + ca[i] + 4096); \
    A[0] = ldstr((sb) + xa[0] + (i) * 4096, (sb) + xa[0] + (i) * 4096 + 1024); \
    A[1] = ldstr((sb) + xa[1] + (i) * 4096, (sb) + xa[1] + (i) * 4096 + 1024); } while (0)
#define U_MMA(A, B) do { \
    acc[0][0] = MFMA(A[0], B[0], acc[0][0]); acc[0][1] = MFMA(A[0], B[1], acc[0][1]); \
    acc[1][0] = MFMA(A[1], B[0], acc[1][0]); acc[1][1] = MFMA(A[1], B[1], acc[1][1]); } while (0)
  asm volatile("s_barrier" ::: "memory");
  STAMP(1);
  U_LOAD(a0, b0, lds0, 0);
  for (int s = 0; s < U_STEPS; ++s) {
    const unsigned sb = lds0 + (unsigned)(s & (U_NSLOT - 1)) * U_SLOT;
    const unsigned sn = lds0 + (unsigned)((s + 1) & (U_NSLOT - 1)) * U_SLOT;
    SBAR();
    U_LOAD(a1, b1, sb, 1); SBAR(); U_MMA(a0, b0); SBAR();
    U_LOAD(a0, b0, sb, 2); SBAR(); U_MMA(a1, b1); SBAR();
    U_LOAD(a1, b1, sb, 3); SBAR(); U_MMA(a0, b0); SBAR();
    if (s + 1 < U_STEPS) { WAITL_BAR(); U_LOAD(a0, b0, sn, 0); }
    SBAR(); U_MMA(a1, b1);
  }
  STAMP(2);
  const float sc = neg ? -1.f : 1.f;
#pragma unroll
  for (int n = 0; n < 2; ++n) {
    const int tt = ((trow0 & 2047) >> 5) + wn * 2 + n;
#pragma unroll
    for (int m = 0; m < 2; ++m) {
      char* dst = ws + ((size_t)(tt * 96 + ksbase + tn * 8 + wm * 4 + m * 2) * 1024) + lane * 16;
      *(u32x4*)dst = natural(pack8<0>(acc[m][n], sc));
      *(u32x4*)(dst + 1024) = natural(pack8<1>(acc[m][n], sc));
    }
  }
}

constexpr int A_SLOT = 45056, A_GW = 3 * A_SLOT, A_K = 0, A_V = 32768, A_ONE = 65536;
constexpr float QSCALE = 0.25f * 1.4426950408889634f;
constexpr float NEGBIG = -1e30f;
DI void a_role(const u16* __restrict__ x, const u16* __restrict__ wqkv, const u16* __restrict__ gw, char* ws, int jb, int hp, unsigned lds0 STAMP_ARG) {
  const int tid = threadIdx.x, lane = tid & 63, wid = __builtin_amdgcn_readfirstlane(tid >> 6);
  const int h = lane >> 5, r32 = lane & 31;
  unsigned vx[4];
#pragma unroll
  for (int i = 0; i < 4; ++i) {
    const int row = wid * 32 + i * 8 + (lane >> 3);
    vx[i] = (unsigned)row * 1024u + (unsigned)(((lane & 7) ^ ((row >> 1) & 7)) * 16);
  }
  const unsigned vkv = (unsigned)((16 * (wid & 3) + (lane >> 2)) * 3072 + (1 + (wid >> 2)) * 1024 + hp * 64 + (lane & 3) * 16);
  const unsigned vq = (unsigned)((8 * wid + (lane >> 2)) * 3072 + hp * 64 + (lane & 3) * 16);
  const char* xh = (const char*)(x + (size_t)(jb > 0 ? jb - 1 : 0) * 256 * 512);
  const char* xo = (const char*)(x + (size_t)jb * 256 * 512);
  auto issue = [&](int s) {
    const unsigned l = lds0 + (unsigned)(s % 3) * A_SLOT;
    const char* xp = (s < 8 ? xh : xo) + (s & 7) * 128;
    const char* wp = (const char*)wqkv + (size_t)(s & 7) * 64 * 3072;
#pragma unroll
    for (int i = 0; i < 4; ++i) dma16(xp, vx[i], l + wid * 4096 + i * 1024);
    dma16(wp, vkv, l + 32768 + 4096 + wid * 1024);
    if (s >= 8) { if (lane < 32) dma16(wp, vq, l + 32768 + wid * 512); }
  };
  if (wid == 0) dma16(gw, lane * 16, lds0 + A_GW);
  issue(0); issue(1);
  unsigned xa[4], wa;
  {
    const int row = wid * 32 + r32;
#pragma unroll
    for (int i = 0; i < 4; ++i) xa[i] = (unsigned)(row * 128 + (((2 * i + h) ^ ((row >> 1) & 7)) * 16));
    const int blk = (lane >> 4) & 1, q = (lane & 15) >> 2, p = lane & 3;
    wa = 32768u + (unsigned)((8 * h + q) * 64 + blk * 32 + p * 8);
  }
  f32x16 aq = {}, ak = {}, av = {};
  u32x4 hk0, hk1, hv0, hv1;
  float gz = 0.f;
#pragma unroll
  for (int s = 0; s < 16; ++s) {
    if (s == 15) WAITVL_BAR(0); else if (s >= 7) WAITVL_BAR(6); else WAITVL_BAR(5);
    if (s == 0) STAMP(1);
    if (s == 8) STAMP(2);
    if (s + 2 < 16) issue(s + 2);
    const unsigned sb = lds0 + (unsigned)(s % 3) * A_SLOT;
#pragma unroll
    for (int i = 0; i < 4; ++i) {
      const bf16x8 xf = lds128(sb + xa[i]);
      const bf16x8 wk = ldstr(sb + wa + 4096 + i * 1024, sb + wa + 4096 + i * 1024 + 256);
      const bf16x8 wv = ldstr(sb + wa + 8192 + i * 1024, sb + wa + 8192 + i * 1024 + 256);
      ak = MFMA(wk, xf, ak);
      av = MFMA(xf, wv, av);
      if (s >= 8) {
        const bf16x8 wq = ldstr(sb + wa + i * 1024, sb + wa + i * 1024 + 256);
        aq = MFMA(wq, xf, aq);
        const bf16x8 gf = lds128(lds0 + A_GW + (s - 8) * 128 + i * 32 + h * 16);
#pragma unroll
        for (int e = 0; e < 4; ++e) {
          bf16x2 u = {xf[2 * e], xf[2 * e + 1]}, v = {gf[2 * e], gf[2 * e + 1]};
          gz = __builtin_amdgcn_fdot2_f32_bf16(u, v, gz, false);
        }
      }
    }
    if (s >= 8) asm volatile("" : "+v"(gz));
    if (s == 7) { hk0 = pack8<0>(ak); hk1 = pack8<1>(ak); hv0 = pack8<0>(av); hv1 = pack8<1>(av); ak = f32x16{}; av = f32x16{}; }
  }
  WAITL_BAR();
  {
    const unsigned kb = lds0 + A_K + lane * 16, vb = lds0 + A_V + lane * 16;
    *(AS3 u32x4*)(kb + wid * 2048) = hk0; *(AS3 u32x4*)(kb + wid * 2048 + 1024) = hk1;
    *(AS3 u32x4*)(vb + wid * 2048) = hv0; *(AS3 u32x4*)(vb + wid * 2048 + 1024) = hv1;
    *(AS3 u32x4*)(kb + (wid + 8) * 2048) = pack8<0>(ak); *(AS3 u32x4*)(kb + (wid + 8) * 2048 + 1024) = pack8<1>(ak);
    *(AS3 u32x4*)(vb + (wid + 8) * 2048) = pack8<0>(av); *(AS3 u32x4*)(vb + (wid + 8) * 2048 + 1024) = pack8<1>(av);
    *(AS3 unsigned*)(lds0 + A_ONE + tid * 4) = 0x3F803F80u;
  }
  const bf16x8 qf0 = asbf(pack8<0>(aq, QSCALE)), qf1 = asbf(pack8<1>(aq, QSCALE));
  float gate;
  {
    auto rr = __builtin_amdgcn_permlane32_swap(__float_as_uint(gz), __float_as_uint(gz), false, false);
    const float z = __uint_as_float(rr[0]) + __uint_as_float(rr[1]);
    gate = 1.f / (1.f + __expf(-z));
    if (hp == 0 && h == 0) ((float*)(ws + WS_G))[jb * 256 + wid * 32 + r32] = 1.f / (1.f + __expf(z));
  }
  WAITL_BAR();
  STAMP(3);
  const int i0 = (jb == 0) ? 8 - wid : 0;
  const unsigned kb = lds0 + A_K + wid * 2048 + lane * 16;
#pragma unroll
  for (int hd = 0; hd < 2; ++hd) {
    const bf16x8 qf = hd ? qf1 : qf0;
    float m = NEGBIG;
#pragma unroll
    for (int i = 0; i < 9; ++i) {
      if (i >= i0) {
        const bf16x8 kf = lds128(kb + i * 2048 + hd * 1024);
        f32x16 sc = MFMA(kf, qf, f32x16{});
        if (i == 0 || i == 8) {
#pragma unroll
          for (int r = 0; r < 16; ++r) { const int key = (r & 3) + 8 * (r >> 2) + 4 * h; const bool ok = (i == 0) ? (key > r32) : (key <= r32); sc[r] = ok ? sc[r] : NEGBIG; }
        }
#pragma unroll
        for (int r = 0; r < 16; r += 2) m = __builtin_fmaxf(__builtin_fmaxf(m, sc[r]), sc[r + 1]);
      }
    }
    { auto rr = __builtin_amdgcn_permlane32_swap(__float_as_uint(m), __float_as_uint(m), false, false);
      m = __builtin_fmaxf(__uint_as_float(rr[0]), __uint_as_float(rr[1])); }
    f32x16 negm;
#pragma unroll
    for (int r = 0; r < 16; ++r) negm[r] = -m;
    const bool own = (r32 >> 4) == hd;
    unsigned va = own ? (lds0 + A_V + wid * 2048 + lane * 16) : (lds0 + A_ONE + lane * 16);
    const unsigned vinc = own ? 2048u : 0u;
    f32x16 o = {};
#pragma unroll
    for (int i = 0; i < 9; ++i) {
      if (i >= i0) {
        const bf16x8 kf = lds128(kb + i * 2048 + hd * 1024);
        const bf16x8 v0 = lds128(va), v1 = lds128(va + 1024);
        f32x16 sc = MFMA(kf, qf, negm);
#pragma unroll
        for (int r = 0; r < 16; ++r) sc[r] = __builtin_amdgcn_exp2f(sc[r]);
        if (i == 0 || i == 8) {
#pragma unroll
          for (int r = 0; r < 16; ++r) { const int key = (r & 3) + 8 * (r >> 2) + 4 * h; const bool ok = (i == 0) ? (key > r32) : (key <= r32); sc[r] = ok ? sc[r] : 0.f; }
        }
        o = MFMA(v0, asbf(pack8<0>(sc)), o);
        o = MFMA(v1, asbf(pack8<1>(sc)), o);
      }
      va += vinc;
    }
    const float l = hd ? o[0] : o[8];
    const float f = gate / l;
    const u32x4 of = hd ? pack8<1>(o, f) : pack8<0>(o, f);
    *(u32x4*)(ws + (size_t)((jb * 8 + wid) * 96 + 64 + hp * 2 + hd) * 1024 + lane * 16) = natural(of);
  }
}

__global__ __launch_bounds__(512, 2) void k1(const u16* __restrict__ x, const u16* __restrict__ wqkv, const u16* __restrict__ cr, const u16* __restrict__ ci,
                                             const u16* __restrict__ gw, char* ws) {
  extern __shared__ __attribute__((aligned(16))) char smem[];
  const unsigned lds0 = (unsigned)(uintptr_t)smem;
  STAMP_DECL;
  STAMP(0);
  const int xcd = blockIdx.x & 7, idx = blockIdx.x >> 3;
  if (idx < 16) {
    const int tm = idx >> 2, tn = idx & 3;
    u_role(x, (tm & 2) ? ci : cr, ws, xcd * 256 + (tm & 1) * 128, tn, ((tm + tn) & 3) * 8, (tm & 2) ? 32 : 0, (tm & 2) != 0, lds0 STAMP_PASS);
  } else {
    a_role(x, wqkv, gw, ws, xcd, idx - 16, lds0 STAMP_PASS);
  }
  STAMP(7);
  STAMP_FLUSH(0);
}

constexpr int F_SLOT = 32768, F_NSLOT = 4, F_STEPS = 12;
__global__ __launch_bounds__(512, 2) void k2(const u16* __restrict__ wo, const u16* __restrict__ dr, const u16* __restrict__ di, char* ws, u16* __restrict__ out) {
  extern __shared__ __attribute__((aligned(16))) char smem[];
  const unsigned lds0 = (unsigned)(uintptr_t)smem;
  STAMP_DECL;
  STAMP(0);
  const int tid = threadIdx.x, lane = tid & 63, wid = __builtin_amdgcn_readfirstlane(tid >> 6);
  const int xcd = blockIdx.x & 7, idx = blockIdx.x >> 3;
  const int tp = xcd * 4 + (idx >> 3), e0 = (idx & 7) * 64;
  if (wid >= 4) {
    const int lw = wid - 4;
    const char* abase = ws + (size_t)((tp * 2 + (lw >> 1)) * 96 + (lw & 1) * 4) * 1024;
    unsigned vd[4], vw[4];
#pragma unroll
    for (int i = 0; i < 4; ++i) {
      const int row = (lw & 1) * 32 + i * 8 + (lane >> 3);
      vd[i] = (unsigned)((e0 + row) * 1024 + (lw >> 1) * 128 + (((lane & 7) ^ ((row >> 1) & 7)) * 16));
      vw[i] = (unsigned)(((lw >> 1) * 64 + row) * 1024 + e0 * 2 + (((lane & 7) ^ (((row >> 1) & 1) << 2)) * 16));
    }
    auto issue = [&](int s) {
      const unsigned l = lds0 + (unsigned)(s & (F_NSLOT - 1)) * F_SLOT + lw * 4096;
      const char* ap = abase + s * 8192;
#pragma unroll
      for (int i = 0; i < 4; ++i) dma16(ap, lane * 16 + i * 1024, l + i * 1024);
      if (s < 8) {
        const char* wp = (const char*)(s < 4 ? dr : di) + (s & 3) * 256;
#pragma unroll
        for (int i = 0; i < 4; ++i) dma16(wp, vd[i], l + 16384 + i * 1024);
      } else {
        const char* wp = (const char*)wo + (size_t)(s - 8) * 128 * 1024;
#pragma unroll
        for (int i = 0; i < 4; ++i) dma16(wp, vw[i], l + 16384 + i * 1024);
      }
    };
    issue(0); issue(1); issue(2);
#pragma unroll
    for (int s = 0; s < F_STEPS - 3; ++s) { WAITV_BAR(16); issue(s + 3); }
    WAITV_BAR(16); WAITV_BAR(8); WAITV_BAR(0);
    return;
  }
  const int we = wid & 1, wt = wid >> 1, h = lane >> 5, r32 = lane & 31;
  const float omg = ((const float*)(ws + WS_G))[tp * 64 + wt * 32 + r32];
  unsigned da[4], wa;
  {
    const int row = we * 32 + r32;
#pragma unroll
    for (int i = 0; i < 4; ++i) da[i] = 16384u + (unsigned)(row * 128 + (((2 * i + h) ^ ((row >> 1) & 7)) * 16));
    const int blk = (lane >> 4) & 1, q = (lane & 15) >> 2, p = lane & 3;
    wa = 16384u + (unsigned)((8 * h + q) * 128 + (((4 * we + 2 * blk + (p >> 1)) ^ (((q >> 1) & 1) << 2)) * 16) + 8 * (p & 1));
  }
  const unsigned aa = (unsigned)(wt * 8192 + lane * 16);
  f32x16 acc = {};
#pragma unroll
  for (int s = 0; s < F_STEPS; ++s) {
    WAITL_BAR();
    if (s == 0) STAMP(1);
    const unsigned sb = lds0 + (unsigned)(s & (F_NSLOT - 1)) * F_SLOT;
    if (s == 8) {
#pragma unroll
      for (int r = 0; r < 16; ++r) acc[r] *= omg;
    }
#pragma unroll
    for (int i = 0; i < 8; ++i) {
      const bf16x8 bf = lds128(sb + aa + i * 1024);
      bf16x8 af;
      if (s < 8) af = lds128(sb + da[i & 3] + (i >> 2) * 8192);
      else af = ldstr(sb + wa + i * 2048, sb + wa + i * 2048 + 512);
      acc = MFMA(af, bf, acc);
    }
  }
  char* dst = (char*)(out + (size_t)(tp * 64 + wt * 32 + r32) * 512 + e0 + we * 32 + h * 8);
  *(u32x4*)dst = natural(pack8<0>(acc));
  *(u32x4*)(dst + 32) = natural(pack8<1>(acc));
  STAMP(7); STAMP_FLUSH(1);
}

extern "C" void kernel_launch(void* const* d_in, const int* in_sizes, int n_in, void* d_out, int out_size, void* d_ws, size_t ws_size, hipStream_t stream) {
  const u16* x = (const u16*)d_in[0]; const u16* wqkv = (const u16*)d_in[1]; const u16* wo = (const u16*)d_in[2];
  const u16* cr = (const u16*)d_in[4]; const u16* ci = (const u16*)d_in[5]; const u16* dr = (const u16*)d_in[6]; const u16* di = (const u16*)d_in[7];
  const u16* gw = (const u16*)d_in[8];
  constexpr int L1B = A_GW + 1024, L2B = F_SLOT * F_NSLOT;
  (void)hipFuncSetAttribute((const void*)k1, hipFuncAttributeMaxDynamicSharedMemorySize, L1B);
  (void)hipFuncSetAttribute((const void*)k2, hipFuncAttributeMaxDynamicSharedMemorySize, L2B);
  k1<<<256, 512, L1B, stream>>>(x, wqkv, cr, ci, gw, (char*)d_ws);
  k2<<<256, 512, L2B, stream>>>(wo, dr, di, (char*)d_ws, (u16*)d_out);
}
```

```cpp
#include <hip/hip_runtime.h>
#include <hip/hip_bf16.h>
#include <stdint.h>
#include <type_traits>
#pragma clang diagnostic ignored "-Wint-to-pointer-cast"

typedef __bf16 bf16x8 __attribute__((ext_vector_type(8)));
typedef __bf16 bf16x4 __attribute__((ext_vector_type(4)));
typedef __bf16 bf16x2 __attribute__((ext_vector_type(2)));
typedef float f32x16 __attribute__((ext_vector_type(16)));
typedef float f32x4 __attribute__((ext_vector_type(4)));
typedef float f32x2 __attribute__((ext_vector_type(2)));
typedef unsigned u32x4 __attribute__((ext_vector_type(4)));
typedef unsigned short u16;
typedef unsigned long long u64x8 __attribute__((ext_vector_type(8)));
typedef unsigned long long u64x2 __attribute__((ext_vector_type(2)));
#define AS3 __attribute__((address_space(3)))
#define AS1 __attribute__((address_space(1)))
#define DI __device__ __forceinline__
#define MFMA(a, b, c) __builtin_amdgcn_mfma_f32_32x32x16_bf16((a), (b), (c), 0, 0, 0)
#define SBAR() __builtin_amdgcn_sched_barrier(0)
#define RLX_AGENT __ATOMIC_RELAXED, __HIP_MEMORY_SCOPE_AGENT

constexpr unsigned WS_ACT = 0;
constexpr unsigned WS_G = 64u * 96 * 1024;
constexpr unsigned WS_REQ = WS_G + 16384, WS_ACK = WS_REQ + 1024, WS_PF = WS_ACK + 1024;
constexpr unsigned WS_KF = WS_PF + 32768;
constexpr unsigned WS_KV = WS_KF + 128 * 128;
constexpr unsigned WS_END = WS_KV + 128 * 32768;
constexpr int UNIT = 32768, LDS_BYTES = 5 * UNIT;
constexpr unsigned SPIN_CAP = 1u << 19;
constexpr int SERVER_BLOCK = 128;
constexpr unsigned CODE_BYTES = 49100;
constexpr int PER = 4;
constexpr int SIGU = 3, POLU = 5;
constexpr int DEPTH = 6;
constexpr float SAFE_HI = 1e36f, SAFE_LO = 1e-20f;

DI void dma16(const void* sbase, unsigned voff, unsigned lds) {
  asm volatile("s_mov_b32 m0, %2\n\ts_nop 0\n\tglobal_load_lds_dwordx4 %0, %1" :: "v"(voff), "s"(sbase), "s"(lds) : "memory");
}
DI void dma4(const void* sbase, const unsigned (&v)[4], unsigned lds) {
  asm volatile("s_mov_b32 m0, %5\n\ts_nop 0\n\tglobal_load_lds_dwordx4 %0, %4\n\tglobal_load_lds_dwordx4 %1, %4 offset:1024\n\t"
               "global_load_lds_dwordx4 %2, %4 offset:2048\n\tglobal_load_lds_dwordx4 %3, %4 offset:3072"
               :: "v"(v[0]), "v"(v[1]), "v"(v[2]), "v"(v[3]), "s"(sbase), "s"(lds) : "memory");
}
DI void dma3(const void* sbase, unsigned v, unsigned lds) {
  asm volatile("s_mov_b32 m0, %2\n\ts_nop 0\n\tglobal_load_lds_dwordx4 %0, %1\n\tglobal_load_lds_dwordx4 %0, %1 offset:1024\n\tglobal_load_lds_dwordx4 %0, %1 offset:2048"
               :: "v"(v), "s"(sbase), "s"(lds) : "memory");
}
template <bool ROWSWZ> DI void piece4(unsigned (&v)[4], int lane) {
  const unsigned c = ROWSWZ ? (unsigned)((lane & 7) ^ (lane >> 4)) : (unsigned)((lane & 7) ^ (((lane >> 4) & 1) << 2));
#pragma unroll
  for (int i = 0; i < 4; ++i) v[i] = (unsigned)((lane >> 3) * 1024 + i * 7168) + ((c * 16) ^ (ROWSWZ ? (i & 1) * 64 : 0));
}
DI bf16x8 lds128(unsigned a) { return *(AS3 const bf16x8*)a; }
DI bf16x4 ldt(unsigned a) { return __builtin_amdgcn_ds_read_tr16_b64_v4bf16((AS3 bf16x4*)a); }
DI bf16x8 cat(bf16x4 lo, bf16x4 hi) { return __builtin_shufflevector(lo, hi, 0, 1, 2, 3, 4, 5, 6, 7); }
DI bf16x8 ldstr(unsigned a0, unsigned a1) { return cat(ldt(a0), ldt(a1)); }
DI unsigned pk2(float a, float b) { f32x2 p = {a, b}; return __builtin_bit_cast(unsigned, __builtin_convertvector(p, bf16x2)); }
template <int s> DI u32x4 pack8(const f32x16& x, float sc) {
  u32x4 r; r[0] = pk2(x[8 * s] * sc, x[8 * s + 1] * sc); r[1] = pk2(x[8 * s + 2] * sc, x[8 * s + 3] * sc);
  r[2] = pk2(x[8 * s + 4] * sc, x[8 * s + 5] * sc); r[3] = pk2(x[8 * s + 6] * sc, x[8 * s + 7] * sc); return r;
}
template <int s> DI u32x4 pack8(const f32x16& x) {
  u32x4 r; r[0] = pk2(x[8 * s], x[8 * s + 1]); r[1] = pk2(x[8 * s + 2], x[8 * s + 3]);
  r[2] = pk2(x[8 * s + 4], x[8 * s + 5]); r[3] = pk2(x[8 * s + 6], x[8 * s + 7]); return r;
}
DI u32x4 natural(u32x4 p) {
  auto a = __builtin_amdgcn_permlane32_swap(p[0], p[2], false, false);
  auto b = __builtin_amdgcn_permlane32_swap(p[1], p[3], false, false);
  u32x4 r; r[0] = a[0]; r[1] = b[0]; r[2] = a[1]; r[3] = b[1]; return r;
}
DI u32x4 join16(f32x4 t0, f32x4 t1) {
  auto a = __builtin_amdgcn_permlane16_swap(pk2(t0[0], t0[1]), pk2(t1[0], t1[1]), false, false);
  auto b = __builtin_amdgcn_permlane16_swap(pk2(t0[2], t0[3]), pk2(t1[2], t1[3]), false, false);
  u32x4 r; r[0] = a[0]; r[1] = b[0]; r[2] = a[1]; r[3] = b[1]; return r;
}
DI bf16x8 asbf(u32x4 v) { return __builtin_bit_cast(bf16x8, v); }
DI float max3(float a, float b, float c) { return __builtin_fmaxf(__builtin_fmaxf(a, b), c); }
#define WAITV_BAR(N) asm volatile("s_waitcnt vmcnt(%0)\n\ts_barrier" :: "i"(N) : "memory")
#define WAITL_BAR() asm volatile("s_waitcnt lgkmcnt(0)\n\ts_barrier" ::: "memory")
#define WAITVL_BAR(N) asm volatile("s_waitcnt vmcnt(%0) lgkmcnt(0)\n\ts_barrier" :: "i"(N) : "memory")
#define LBAR() do { asm volatile("" ::: "memory"); __builtin_amdgcn_s_waitcnt(0xC07F); __builtin_amdgcn_s_barrier(); asm volatile("" ::: "memory"); SBAR(); } while (0)
DI void mma16(f32x4& c, bf16x8 a, bf16x8 b) { asm volatile("v_mfma_f32_16x16x32_bf16 %0, %1, %2, %0" : "+v"(c) : "v"(a), "v"(b)); }
#define DRAIN() asm volatile("s_waitcnt vmcnt(0)" ::: "memory")

struct Ptrs { const u16 *x, *wqkv, *cr, *ci, *gw; char* ws; const u16 *wo, *dr, *di; u16* out; };

DI AS1 unsigned* wsw(char* ws, unsigned off) { return (AS1 unsigned*)(ws + off); }
DI unsigned ldw(char* ws, unsigned off) { return __hip_atomic_load(wsw(ws, off), RLX_AGENT); }
DI void stw(char* ws, unsigned off, unsigned v) { __hip_atomic_store(wsw(ws, off), v, RLX_AGENT); }
DI void raise(char* ws, unsigned off, int lane) { if (lane == 0) stw(ws, off, 1u); }
DI void serve(__amdgpu_buffer_rsrc_t rs, int lane) {
  const u32x4 r = __builtin_amdgcn_raw_buffer_load_b128(rs, WS_REQ + lane * 16, 0, 16);
  __builtin_amdgcn_raw_buffer_store_b128(r, rs, WS_ACK + lane * 16, 0, 16);
}
DI void confirm(char* ws) {
_Pragma("unroll 1")  for (unsigned spins = 0; spins < SPIN_CAP; ++spins) {
    const unsigned a = ldw(ws, WS_ACK + blockIdx.x * 4), r = ldw(ws, WS_REQ + blockIdx.x * 4);
    DRAIN();
    if (__builtin_amdgcn_readfirstlane(a) == __builtin_amdgcn_readfirstlane(r)) break;
    __builtin_amdgcn_s_sleep(2);
  }
}
DI bool last_of(unsigned ldsw, unsigned n, int lane) {
  unsigned c = 0;
  if (lane == 0) c = __hip_atomic_fetch_add((AS3 unsigned*)ldsw, 1u, __ATOMIC_RELAXED, __HIP_MEMORY_SCOPE_WORKGROUP);
  return (unsigned)__builtin_amdgcn_readfirstlane(c) == n - 1;
}
DI void touch(const void* sbase, unsigned voff, unsigned lds) {
  asm volatile("s_mov_b32 m0, %2\n\ts_nop 0\n\tglobal_load_lds_dword %0, %1" :: "v"(voff), "s"(sbase), "s"(lds) : "memory");
}

DI void issue_unit(const Ptrs& p, int u, int e0, int lw, const unsigned (&v)[4], unsigned lds0) {
  const unsigned l = lds0 + (unsigned)(((u + 2) % 5) * UNIT + lw * 8192);
  const char* b = u < 2 ? (const char*)p.wo + (size_t)(u * 256 + lw * 64) * 1024 + e0 * 2
                        : (const char*)(u < 4 ? p.dr : p.di) + (size_t)e0 * 1024 + ((u & 1) * 256 + lw * 64) * 2;
  dma4(b, v, l); dma4(b + 32768, v, l + 4096);
}
DI void load_wof(bf16x8 (&wof)[8][2], int kq, int lane, unsigned lds0) {
  const int h = lane >> 5, blk = (lane >> 4) & 1, q = (lane & 15) >> 2, pp = lane & 3;
  const unsigned wb = lds0 + 2 * UNIT + kq * 16384 + (unsigned)((8 * h + q) * 128 + 8 * (pp & 1));
  const unsigned w0 = wb + (unsigned)(((2 * blk + (pp >> 1)) ^ (((q >> 1) & 1) << 2)) * 16), w1 = w0 ^ 64u;
#pragma unroll
  for (int i = 0; i < 8; ++i) { wof[i][0] = ldstr(w0 + i * 2048, w0 + i * 2048 + 512); wof[i][1] = ldstr(w1 + i * 2048, w1 + i * 2048 + 512); }
}

constexpr int U_STEPS = 32;
constexpr int A_SLOT = 45056, A_GW = 3 * A_SLOT, A_DUMMY = A_GW + 1024, A_K = 0, A_V = UNIT, A_Q = 139264, A_ONE = A_Q + 16384, A_CNT = A_ONE + 2048;
DI int a_jb(int xcd, int idx) { return (xcd & 1) * 4 + ((idx >> 2) & 3); }
DI int a_hp(int xcd, int idx) { return (xcd >> 1) * 4 + (idx & 3); }
struct Loader {
  const char *b1, *b2, *b3; unsigned v1[4], v2[4], v3, l1, l2, l3, o1, o2, o3; int koff, mask, sh, kx;
  DI void bind(const Ptrs& p, bool u, int idx) {
    b1 = (const char*)(u ? ((idx & 8) ? p.ci : p.cr) : p.x) + o1; b2 = (const char*)p.x + o2; b3 = (const char*)p.wqkv + o3;
  }
  DI void init(bool u, int xcd, int idx, int lw, int lane, unsigned lds0) {
    const int tm = idx >> 2, tn = idx & 3, jb = a_jb(xcd, idx), hp = a_hp(xcd, idx);
    const unsigned pitch = u ? 4096u : 1024u;
#pragma unroll
    for (int i = 0; i < 4; ++i) {
      v1[i] = (unsigned)((lane >> 3) + i * 8) * pitch - i * 1024 + (unsigned)((((lane & 7) ^ (lane >> 4)) * 16) ^ ((i & 1) * 64));
      const unsigned vb = (unsigned)((lane >> 4) * 1024 + i * (4096 - 1024)) + (unsigned)((((lane & 15) ^ ((lane >> 4) << 2)) * 16) ^ ((i >> 1) * 32));
      v2[i] = u ? vb : v1[i];
    }
    v3 = (unsigned)((lane >> 2) * 3072 + (((lane & 3) ^ ((lane >> 5) << 1)) * 16));
    const int m = (idx - 16) >> 31;
    auto sel = [&](int a, int b) { return b + ((a - b) & m); };
    o1 = sel((xcd * 256 + (tm & 1) * 128 + lw * 32) * 4096, (jb * 256 + lw * 64) * 1024);
    o2 = sel(lw * 16384 + tn * 256, (jb * 256 + lw * 64 + 32) * 1024);
    o3 = lw * 16 * 3072 + hp * 64;
    l1 = lds0 + sel(lw * 4096, lw * 8192); l2 = l1 + sel(16384, 4096); l3 = lds0 + 32768 + lw * 3072;
    koff = sel(((tm + tn) & 3) * (PER / 4), 2 * hp + (jb & 1) + (jb & 2) * 2); kx = ((~xcd & 1) << 4) & m; mask = sel(PER - 1, 7); sh = sel(16, 7);
    asm volatile("" : "+v"(v1[0]), "+v"(v1[1]), "+v"(v1[2]), "+v"(v1[3]), "+v"(v2[0]), "+v"(v2[1]), "+v"(v2[2]), "+v"(v2[3]), "+v"(v3));
    asm volatile("" : "+s"(l1), "+s"(l2), "+s"(l3), "+s"(o1), "+s"(o2), "+s"(o3), "+s"(koff), "+s"(mask), "+s"(sh), "+s"(kx));
  }
  DI int chunk(int n) const { return ((n & ~mask) | ((n + koff) & mask)) ^ kx; }
  DI void issue(int n, unsigned slot, bool withW) const {
    const int ks = chunk(n);
    if (withW) dma3(b3 + ks * (64 * 3072), v3, l3 + slot);
    dma4(b1 + ks * 128, v1, l1 + slot);
    dma4(b2 + ((unsigned)ks << sh), v2, l2 + slot);
  }
};
DI void u_loader(const Loader& ld) {
  unsigned slot = 2 * UNIT;
#pragma clang loop unroll(disable)
  for (int n = 2; n < U_STEPS; ++n) {
    if (n >= 5) WAITV_BAR(16); else if (n != 4) WAITV_BAR(8);
    ld.issue(n, slot, false);
    slot = (slot == 4 * UNIT) ? 0 : slot + UNIT;
  }
}
DI void u_loader_tail(const Ptrs& p, int e0, int kq, int wid, unsigned lds0, bf16x8 (&wof)[8][2]) {
  const int lane = threadIdx.x & 63, lw = wid - 4;
  unsigned vt[4], vr[4];
  piece4<false>(vt, lane); piece4<true>(vr, lane);
  issue_unit(p, 0, e0, lw, vt, lds0);
  WAITV_BAR(24); issue_unit(p, 1, e0, lw, vt, lds0);
  WAITV_BAR(16);
  issue_unit(p, 2, e0, lw, vr, lds0);
  u32x4 rg[24];
#pragma unroll
  for (int u = 3; u < 6; ++u) {
    const char* b = (const char*)(u < 4 ? p.dr : p.di) + (size_t)e0 * 1024 + ((u & 1) * 256 + lw * 64) * 2;
#pragma unroll
    for (int i = 0; i < 8; ++i) { rg[(u - 3) * 8 + i] = *(AS1 const u32x4*)(b + (i >> 2) * 32768 + (i & 3) * 1024 + vr[i & 3]); SBAR(); }
  }
  WAITV_BAR(32);
  load_wof(wof, kq, lane, lds0);
#pragma unroll
  for (int i = 0; i < 8; ++i) *(AS3 u32x4*)(lds0 + (i >> 3) * UNIT + lw * 8192 + (i & 7) * 1024 + lane * 16) = rg[i];
  WAITL_BAR();
#pragma unroll
  for (int i = 8; i < 24; ++i) *(AS3 u32x4*)(lds0 + (i >> 3) * UNIT + lw * 8192 + (i & 7) * 1024 + lane * 16) = rg[i];
}
DI void u_compute(const Ptrs& p, __amdgpu_buffer_rsrc_t rs, int xcd, int idx, int kq, int wid, unsigned lds0, unsigned ack0, bf16x8 (&wof)[8][2]) {
  char* ws = p.ws;
  const int lane = threadIdx.x & 63;
  const int tm = idx >> 2, tn = idx & 3, trow0 = xcd * 256 + (tm & 1) * 128;
  const int wm = wid & 1, wn = wid >> 1, g4 = lane >> 4, r16 = lane & 15;
  unsigned ofs[6], adr[6];
  {
    const int q = r16 >> 2, pp = lane & 3;
#pragma unroll
    for (int m = 0; m < 4; ++m) ofs[2 + m] = lds0 + 16384u + (unsigned)((8 * g4 + q) * 256 + (((8 * wm + 2 * m + (pp >> 1)) ^ (q << 2) ^ ((g4 & 1) << 1)) * 16) + 8 * (pp & 1));
    const int row = wn * 64 + r16;
#pragma unroll
    for (int i = 0; i < 2; ++i) ofs[i] = lds0 + (unsigned)(row * 128 + (((4 * i + g4) ^ ((row >> 1) & 7)) * 16));
  }
  f32x4 acc[4][4] = {};
#pragma unroll
  for (int m = 0; m < 4; ++m) asm volatile("" : "+v"(acc[m][0]), "+v"(acc[m][1]), "+v"(acc[m][2]), "+v"(acc[m][3]));
  bf16x8 bA[2][4], bB[2][4];
  bf16x4 aA[2][4][2], aB[2][4][2];
#define V_RD(A, B, r) do { constexpr int i_ = (r) / 12, j_ = (r) % 12; \
    if (j_ < 4) B[i_][j_ & 3] = lds128(adr[i_] + (j_ & 3) * 2048); \
    else A[i_][((j_ - 4) >> 1) & 3][j_ & 1] = ldt(adr[2 + (((j_ - 4) >> 1) & 3)] + i_ * 8192 + (j_ & 1) * 1024); } while (0)
#define V_MM(A, B, g) do { constexpr int i_ = (g) >> 4, m_ = ((g) >> 2) & 3, n_ = (m_ & 1) ? 3 - ((g) & 3) : ((g) & 3); \
    mma16(acc[m_][n_], cat(A[i_][m_][0], A[i_][m_][1]), B[i_][n_]); } while (0)
#define V_ML(A, B, g) do { constexpr int i_ = ((g) ^ ((g) >> 1)) & 1, m_ = ((g) >> 3) & 3, n_ = ((g) >> 1) & 3; \
    mma16(acc[m_][n_], cat(A[i_][m_][0], A[i_][m_][1]), B[i_][n_]); } while (0)
#define V_AD(k) do { adr[k] = ofs[k] + slot; asm volatile("" : "+v"(adr[k])); } while (0)
#define V_G(CA, CB, NA, NB, g) V_ML(CA, CB, g); V_RD(NA, NB, g); SBAR();
#define V_G4(CA, CB, NA, NB, g) V_G(CA, CB, NA, NB, g) V_G(CA, CB, NA, NB, g + 1) V_G(CA, CB, NA, NB, g + 2) V_G(CA, CB, NA, NB, g + 3)
#define V_M4(CA, CB, g) V_MM(CA, CB, g); V_MM(CA, CB, g + 1); V_MM(CA, CB, g + 2); V_MM(CA, CB, g + 3);
#define V_R4(NA, NB, g) V_RD(NA, NB, g); V_RD(NA, NB, g + 1); V_RD(NA, NB, g + 2); V_RD(NA, NB, g + 3);
#define V_HEAD(CA, CB, NA, NB) V_G4(CA, CB, NA, NB, 0) V_G4(CA, CB, NA, NB, 4) V_G4(CA, CB, NA, NB, 8) V_G4(CA, CB, NA, NB, 12) V_G4(CA, CB, NA, NB, 16) V_G4(CA, CB, NA, NB, 20)
#define V_STEP(CA, CB, NA, NB) do { \
    V_HEAD(CA, CB, NA, NB) \
    slot = (slot == 4 * UNIT) ? 0 : slot + UNIT; \
    V_ML(CA, CB, 24); V_AD(0); SBAR(); V_ML(CA, CB, 25); V_AD(1); SBAR(); V_ML(CA, CB, 26); V_AD(2); SBAR(); V_ML(CA, CB, 27); V_AD(3); SBAR(); \
    V_ML(CA, CB, 28); V_AD(4); SBAR(); V_ML(CA, CB, 29); V_AD(5); SBAR(); V_ML(CA, CB, 30); V_ML(CA, CB, 31); SBAR(); } while (0)
  unsigned slot = 0;
  V_AD(0); V_AD(1); V_AD(2); V_AD(3); V_AD(4); V_AD(5);
  __builtin_amdgcn_s_waitcnt(0x0F70); DRAIN();
  asm volatile("" : "+v"(ack0));
  if (threadIdx.x == 0) stw(p.ws, WS_REQ + blockIdx.x * 4, ack0 + 1u);
  asm volatile("s_barrier" ::: "memory");
  V_R4(aA, bA, 0) V_R4(aA, bA, 4) V_R4(aA, bA, 8) V_R4(aA, bA, 12) V_R4(aA, bA, 16) V_R4(aA, bA, 20)
  SBAR();
  slot = UNIT;
  V_AD(0); V_AD(1); V_AD(2); V_AD(3); V_AD(4); V_AD(5);
#pragma clang loop unroll(disable)
  for (int s = 0; s < U_STEPS - 2; s += 2) {
    LBAR(); V_STEP(aA, bA, aB, bB);
    LBAR(); V_STEP(aB, bB, aA, bA);
  }
  __builtin_amdgcn_s_waitcnt(0xC07F); SBAR();
  V_STEP(aA, bA, aB, bB);
  __builtin_amdgcn_s_waitcnt(0x0070);
  DRAIN();
  SBAR();
  const float sc = (tm & 2) ? -1.f : 1.f;
  const unsigned so = (unsigned)(((trow0 >> 5) + wn * 2) * 96 + (tm & 2) * 16 + tn * 8 + wm * 4) * 1024u + lane * 16;
#define V_BAND(m) V_M4(aB, bB, 4 * (m)) V_M4(aB, bB, 16 + 4 * (m))
#define V_PIN(m) asm volatile("" : "+v"(acc[m][0]), "+v"(acc[m][1]), "+v"(acc[m][2]), "+v"(acc[m][3]))
#define V_OUT(m, n) __builtin_amdgcn_raw_buffer_store_b128(join16(acc[m][2 * (n)] * sc, acc[m][2 * (n) + 1] * sc), rs, so + (n) * (96 * 1024) + (m) * 1024, 0, 16)
  V_BAND(0) V_BAND(1) V_PIN(0); SBAR();
  V_M4(aB, bB, 8) V_OUT(0, 0); SBAR(); V_M4(aB, bB, 24) V_OUT(0, 1); V_PIN(1); SBAR();
  V_M4(aB, bB, 12) V_OUT(1, 0); SBAR(); V_M4(aB, bB, 28) V_OUT(1, 1); V_PIN(2); SBAR();
  V_OUT(2, 0); V_OUT(2, 1); SBAR();
  asm volatile("s_nop 7" : "+v"(acc[3][0]), "+v"(acc[3][1]), "+v"(acc[3][2]), "+v"(acc[3][3]));
  V_OUT(3, 0); V_OUT(3, 1); SBAR();
  asm volatile("s_barrier" ::: "memory");
  load_wof(wof, kq, lane, lds0);
  if (wid == 0) confirm(ws); else DRAIN();
  WAITL_BAR();
  if (wid == 0) raise(ws, WS_PF + (((xcd * 2 + (tm & 1)) * 2 + (tm >> 1)) * 4 + tn) * 128, lane);
}

constexpr float QSCALE = 0.25f * 1.4426950408889634f;
constexpr float NEGBIG = -1e30f;
DI float xhalf_max(float m) {
  auto r = __builtin_amdgcn_permlane32_swap(__float_as_uint(m), __float_as_uint(m), false, false);
  return __builtin_fmaxf(__uint_as_float(r[0]), __uint_as_float(r[1]));
}
DI f32x16 splat16(float v) { f32x16 r;
#pragma unroll
  for (int i = 0; i < 16; ++i) r[i] = v;
  return r; }
DI bool upper(int r, int h, int r32) { return ((r & 3) + 8 * (r >> 2) + 4 * h) > r32; }
template <int N, class F> DI void sfor(F&& f) { if constexpr (N > 0) { sfor<N - 1>(f); f(std::integral_constant<int, N - 1>{}); } }

DI float max16(const f32x16& x) {
  float m0 = max3(x[0], x[1], x[2]), m1 = max3(x[3], x[4], x[5]);
  m0 = max3(m0, x[6], x[7]); m1 = max3(m1, x[8], x[9]); m0 = max3(m0, x[10], x[11]); m1 = max3(m1, x[12], x[13]);
  return max3(max3(m0, x[14], x[15]), m1, m1);
}
template <bool DIAGT> DI f32x16 tri16(float nm, int h, int r32) { f32x16 r;
#pragma unroll
  for (int i = 0; i < 16; ++i) r[i] = (upper(i, h, r32) == DIAGT) ? NEGBIG : nm;
  return r; }
constexpr int pv_tile(int u, int NT, int S0, int NS) { int c = 0; for (int j = 0; j < NT; ++j) { if (c == u) return j; ++c; if (j >= S0 && j < S0 + NS) { if (c == u) return j; ++c; } } return -1; }
constexpr bool pv_big(int u, int NT, int S0, int NS) { return u == 0 || pv_tile(u, NT, S0, NS) != pv_tile(u - 1, NT, S0, NS); }
template <int NT, int MB, int S0, int NS, int MS, bool DIAGT, class H>
DI void att_pv(unsigned ka, unsigned va, unsigned vs, bf16x8 qbig, bf16x8 qsm, const f32x16& nm, f32x16& obig, f32x16& osm, int h, int r32, H&& hook) {
  f32x16 sc[2]; bf16x8 kf[3], v0[2], v1[2]; u32x4 p1;
  auto issue = [&](auto uc) { constexpr int u = uc, j = pv_tile(u, NT, S0, NS); constexpr bool big = pv_big(u, NT, S0, NS);
    if constexpr (j == (big ? MB : MS)) sc[u & 1] = MFMA(kf[j % 3], big ? qbig : qsm, tri16<DIAGT>(nm[0], h, r32));
    else sc[u & 1] = MFMA(kf[j % 3], big ? qbig : qsm, nm); };
  kf[0] = lds128(ka); kf[1] = lds128(ka + 2048);
  v0[0] = lds128(va); v1[0] = lds128(va + 1024);
  issue(std::integral_constant<int, 0>{});
  sfor<9>([&](auto uc) { constexpr int u = uc, j = pv_tile(u, NT, S0, NS);
    constexpr bool big = pv_big(u, NT, S0, NS);
    const f32x16& x = sc[u & 1]; f32x16 e;
    SBAR();
#pragma unroll
    for (int r = 0; r < 2; ++r) e[r] = __builtin_amdgcn_exp2f(x[r]);
    SBAR();
    if constexpr (u > 0) { constexpr int jp = pv_tile(u - 1, NT, S0, NS); f32x16& o = pv_big(u - 1, NT, S0, NS) ? obig : osm; o = MFMA(v1[jp & 1], asbf(p1), o); }
    if constexpr (big && j + 2 < NT) kf[(j + 2) % 3] = lds128(ka + (j + 2) * 2048);
    if constexpr (big && j + 1 < NT) { va += vs; v0[(j + 1) & 1] = lds128(va); v1[(j + 1) & 1] = lds128(va + 1024); }
    SBAR();
#pragma unroll
    for (int r = 2; r < 8; ++r) e[r] = __builtin_amdgcn_exp2f(x[r]);
    SBAR();
    if constexpr (u < 8) issue(std::integral_constant<int, u + 1>{});
    hook(uc);
    SBAR();
    const u32x4 p0 = pack8<0>(e);
    SBAR();
#pragma unroll
    for (int r = 8; r < 12; ++r) e[r] = __builtin_amdgcn_exp2f(x[r]);
    SBAR();
    { f32x16& o = big ? obig : osm; o = MFMA(v0[j & 1], asbf(p0), o); }
    SBAR();
#pragma unroll
    for (int r = 12; r < 16; ++r) e[r] = __builtin_amdgcn_exp2f(x[r]);
    p1 = pack8<1>(e);
  });
  SBAR();
  { constexpr int jp = pv_tile(8, NT, S0, NS); f32x16& o = pv_big(8, NT, S0, NS) ? obig : osm; o = MFMA(v1[jp & 1], asbf(p1), o); }
}
DI f32x16 att_exact(unsigned ka, unsigned va, unsigned vs, int i0, bf16x8 q, int h, int r32) {
  float m = NEGBIG;
_Pragma("unroll 1")  for (int i = i0; i < 9; ++i) {
    f32x16 c; const float cu = (i == 8) ? NEGBIG : 0.f, cl = (i == 0) ? NEGBIG : 0.f;
#pragma unroll
    for (int r = 0; r < 16; ++r) c[r] = upper(r, h, r32) ? cu : cl;
    m = __builtin_fmaxf(m, max16(MFMA(lds128(ka + i * 2048), q, c)));
  }
  m = xhalf_max(m);
  f32x16 o = {};
_Pragma("unroll 1")  for (int i = i0; i < 9; ++i) {
    f32x16 c; const float cu = (i == 8) ? NEGBIG : -m, cl = (i == 0) ? NEGBIG : -m;
#pragma unroll
    for (int r = 0; r < 16; ++r) c[r] = upper(r, h, r32) ? cu : cl;
    f32x16 x = MFMA(lds128(ka + i * 2048), q, c);
#pragma unroll
    for (int r = 0; r < 16; ++r) x[r] = __builtin_amdgcn_exp2f(x[r]);
    o = MFMA(lds128(va + i * vs), asbf(pack8<0>(x)), o);
    o = MFMA(lds128(va + i * vs + 1024), asbf(pack8<1>(x)), o);
  }
  return o;
}

template <int QB>
DI void attention(const Ptrs& p, __amdgpu_buffer_rsrc_t rs, int jb, int hp, int e0, int wid, int kq, int flip, unsigned lds0, unsigned kb, unsigned vb, unsigned vs,
                  bf16x8 fa, bf16x8 fb, f32x16& oa, f32x16& ob, bf16x8 (&wof)[8][2]) {
  constexpr int QA = 7 - QB;
  char* ws = p.ws;
  const int lane = threadIdx.x & 63, h = lane >> 5, r32 = lane & 31, hd = wid >> 2;
  f32x16 nm;
  {
    const f32x16 ml = tri16<true>(0.f, h, r32);
    const f32x16 sa = MFMA(lds128(kb + (8 + QA) * 2048), fa, ml), sb = MFMA(lds128(kb + (8 + QB) * 2048), fb, ml);
    nm = splat16(-xhalf_max(__builtin_fmaxf(max16(sa), max16(sb))));
  }
  const unsigned fo = WS_KF + ((jb > 0 ? jb - 1 : 0) * 16 + hp) * 128;
  const unsigned so = WS_KV + (unsigned)((jb > 0 ? jb - 1 : 0) * 16 + hp) * 32768u + wid * 4096 + lane * 16;
  const unsigned wl = lds0 + 2 * UNIT + wid * 8192;
  u32x4 kv[4]; unsigned flag = 0; bool got = false;
  att_pv<QA + 1, QA, 0, QB + 1, QB, true>(kb + 16384, vb + 8 * vs, vs, fa, fb, nm, oa, ob, h, r32, [&](auto uc) { constexpr int u = uc;
    if constexpr (u == SIGU) {
      if (wid < 4) {
        DRAIN();
        if (last_of(lds0 + A_CNT, 4, lane)) raise(ws, WS_KF + (jb * 16 + hp) * 128, lane);
      }
    }
    if constexpr (u == SIGU || u == SIGU + 1) {
      unsigned vt[4]; piece4<false>(vt, lane);
      dma4((const char*)p.wo + (size_t)(wid * 64) * 1024 + e0 * 2 + (u - SIGU) * 32768, vt, wl + (u - SIGU) * 4096);
    }
    if constexpr (u == POLU) flag = ldw(ws, fo);
    if constexpr (u == POLU + 2) {
      got = __builtin_amdgcn_readfirstlane(flag) != 0;
      if (got) {
        asm volatile("" ::: "memory");
#pragma unroll
        for (int i = 0; i < 4; ++i) kv[i] = __builtin_amdgcn_raw_buffer_load_b128(rs, so + i * 1024, 0, 16);
      }
    } });
  if (jb > 0) {
    if (!got) {
_Pragma("unroll 1")      for (unsigned spins = 0; spins < SPIN_CAP; ++spins) {
        if (__builtin_amdgcn_readfirstlane(ldw(ws, fo)) != 0) break;
        __builtin_amdgcn_s_sleep(4);
      }
      asm volatile("" ::: "memory");
#pragma unroll
      for (int i = 0; i < 4; ++i) kv[i] = __builtin_amdgcn_raw_buffer_load_b128(rs, so + i * 1024, 0, 16);
    }
    const unsigned d = lds0 + hd * UNIT + QB * 4096 + lane * 16;
#pragma unroll
    for (int i = 0; i < 4; ++i) *(AS3 u32x4*)(d + i * 1024) = kv[i];
  }
  __builtin_amdgcn_s_waitcnt(0x0070);
  WAITVL_BAR(0);
  touch(wid < 4 ? p.dr : p.di, (unsigned)((((blockIdx.x >> 3) & 15) * 4 + QB) * 8192 + lane * 128), lds0 + A_DUMMY);
  load_wof(wof, kq, lane, lds0);
  const char* dg = (const char*)(flip ? p.di : p.dr) + (size_t)e0 * 1024 + wid * 128;
  const unsigned dv = (unsigned)((lane >> 3) * 1024 + (((lane & 7) ^ (lane >> 4)) * 16));
  if (jb == 0) {
    WAITL_BAR();
#pragma unroll
    for (int i = 0; i < 8; ++i) dma16(dg + i * 8192, dv ^ ((i & 1) * 64), wl + i * 1024);
  }
  if (jb > 0)
    att_pv<QA + 1, 0, QA - QB, QB + 1, QA - QB, false>(kb + QB * 2048, vb + QB * vs, vs, fb, fa, nm, ob, oa, h, r32, [&](auto uc) { constexpr int u = uc;
      if constexpr (u == 0) WAITL_BAR();
      if constexpr (u < 8) dma16(dg + u * 8192, dv ^ ((u & 1) * 64), wl + u * 1024); });
}

DI void a_loader(const Ptrs& p, const Loader& ld, __amdgpu_buffer_rsrc_t rs, int jb, int hp, int wid, unsigned lds0) {
  char* ws = p.ws;
  const int tid = threadIdx.x, lane = tid & 63, lw = wid - 4;
  *(AS3 unsigned long long*)(lds0 + A_ONE + (lw * 64 + lane) * 8) = ((lane >> 1) & 11) == 0 ? 0x3F803F803F803F80ull : 0ull;
  if (tid == 256) *(AS3 unsigned long long*)(lds0 + A_CNT) = 0ull;
  const int grow = hp * 16 + (lane >> 2);
  const unsigned ga = (unsigned)(grow * 128 + ((((lane & 3) * 2) ^ ((grow >> 1) & 7)) * 16)), gc = (unsigned)((lane & 3) * 32);
  float gz = 0.f; bool conf = false;
  unsigned cur = A_SLOT, nxt = 0;
#pragma clang loop unroll(disable)
  for (int s = 0; s < 8; ++s) {
    if (s == 7) WAITVL_BAR(1); else WAITVL_BAR(11);
    if (s < 6) ld.issue(s + 2, nxt, true);
    if (lw == 3) {
      const unsigned sb = lds0 + cur, gb = lds0 + A_GW + (unsigned)ld.chunk(s) * 128 + gc;
#pragma unroll
      for (int c = 0; c < 2; ++c) {
        const bf16x8 xf = lds128(sb + (ga ^ (c * 16))), gf = lds128(gb + c * 16);
#pragma unroll
        for (int e = 0; e < 4; ++e) { bf16x2 u = {xf[2 * e], xf[2 * e + 1]}, v = {gf[2 * e], gf[2 * e + 1]}; gz = __builtin_amdgcn_fdot2_f32_bf16(u, v, gz, false); }
      }
      asm volatile("" : "+v"(gz));
      if (s == 7) {
        gz += __shfl_xor(gz, 1); gz += __shfl_xor(gz, 2);
        if ((lane & 3) < 2)
          __builtin_amdgcn_raw_buffer_store_b32(__float_as_uint(1.f / (1.f + __expf((lane & 1) ? gz : -gz))), rs, WS_G + (jb * 16 + hp) * 128 + (lane & 1) * 64 + (lane & 60), 0, 16);
      }
    }
    if (lw == 0) {
      if (s == 6) { const unsigned a = ldw(ws, WS_ACK + blockIdx.x * 4), r = ldw(ws, WS_REQ + blockIdx.x * 4); conf = __builtin_amdgcn_readfirstlane(a) == __builtin_amdgcn_readfirstlane(r); }
      if (s == 7 && !conf) confirm(ws);
    }
    if (s >= 6) {
      const int xcd = blockIdx.x & 7, pr = (((blockIdx.x >> 3) & 15) * 4 + lw) * 8;
      touch((const char*)((pr & 256) ? p.ci : p.cr) + (size_t)(xcd * 256 + (pr & 255)) * 4096 + (((s - 4) ^ ((~xcd & 1) * 2)) * 1024), (unsigned)((lane >> 3) * 4096 + (lane & 7) * 128), lds0 + A_DUMMY);
    }
    cur = (cur == 2 * A_SLOT) ? 0 : cur + A_SLOT; nxt = (nxt == 2 * A_SLOT) ? 0 : nxt + A_SLOT;
  }
  WAITL_BAR();
}
DI void a_compute(const Ptrs& p, __amdgpu_buffer_rsrc_t rs, int jb, int hp, int wid, bool server, unsigned lds0, unsigned ack0) {
  const int lane = threadIdx.x & 63;
  const bool srv = server && wid == 0;
  {
    const int g4 = lane >> 4, r16 = lane & 15;
    unsigned ofs[4], adr[4];
    {
#pragma unroll
      for (int i = 0; i < 2; ++i) ofs[i] = lds0 + (unsigned)((wid * 64 + r16) * 128 + (((4 * i + g4) ^ ((r16 >> 1) & 7)) * 16));
      const int q = r16 >> 2, pp = lane & 3;
#pragma unroll
      for (int c = 0; c < 2; ++c) ofs[2 + c] = lds0 + 32768u + (unsigned)((g4 >> 1) * 3072 + (8 * (g4 & 1) + q) * 64 + (((2 * c + (pp >> 1)) ^ ((g4 & 1) << 1)) * 16) + 8 * (pp & 1));
    }
    f32x4 acc[6][4] = {};
#pragma unroll
    for (int m = 0; m < 6; ++m) asm volatile("" : "+v"(acc[m][0]), "+v"(acc[m][1]), "+v"(acc[m][2]), "+v"(acc[m][3]));
    bf16x8 x0[4], x1[4];
    bf16x4 w0[6][2], w1[6][2];
    u32x4 rq = {};
#define Q_RD(X, W, i, r) do { \
    if ((r) < 4) X[(r) & 3] = lds128(adr[i] + ((r) & 3) * 2048); \
    else if ((r) < 16) { constexpr int f_ = (((r) - 4) >> 1) % 6; W[f_][(r) & 1] = ldt(adr[2 + (f_ & 1)] + (((f_ >> 1) + 1) % 3) * 1024 + (i) * 6144 + ((r) & 1) * 256); } } while (0)
#define Q_MM(X, W, g) do { constexpr int f_ = (g) >> 2, a_ = (f_ & 1) ? 3 - ((g) & 3) : ((g) & 3); \
    if ((f_ >> 1) == 1) mma16(acc[f_][a_], X[a_], cat(W[f_][0], W[f_][1])); else mma16(acc[f_][a_], cat(W[f_][0], W[f_][1]), X[a_]); } while (0)
#define Q_AD(k) do { adr[k] = ofs[k] + cur; asm volatile("" : "+v"(adr[k])); } while (0)
#define Q_G(CX, CW, NX, NW, i, g) Q_MM(CX, CW, g); Q_RD(NX, NW, i, g); SBAR();
#define Q_G4(CX, CW, NX, NW, i, g) Q_G(CX, CW, NX, NW, i, g) Q_G(CX, CW, NX, NW, i, g + 1) Q_G(CX, CW, NX, NW, i, g + 2) Q_G(CX, CW, NX, NW, i, g + 3)
#define Q_M4(CX, CW, g) Q_MM(CX, CW, g); Q_MM(CX, CW, g + 1); Q_MM(CX, CW, g + 2); Q_MM(CX, CW, g + 3);
#define Q_R4(NX, NW, i, g) Q_RD(NX, NW, i, g); SBAR(); Q_RD(NX, NW, i, g + 1); SBAR(); Q_RD(NX, NW, i, g + 2); SBAR(); Q_RD(NX, NW, i, g + 3); SBAR();
#define Q_HEAD(CX, CW, NX, NW, i) Q_G4(CX, CW, NX, NW, i, 0) Q_G4(CX, CW, NX, NW, i, 4) Q_G4(CX, CW, NX, NW, i, 8) Q_G4(CX, CW, NX, NW, i, 12)
    unsigned cur = A_SLOT;
    Q_AD(0); Q_AD(1); Q_AD(2); Q_AD(3);
    __builtin_amdgcn_s_waitcnt(0x0F70); DRAIN();
    asm volatile("" : "+v"(ack0));
    if (threadIdx.x == 0) stw(p.ws, WS_REQ + blockIdx.x * 4, ack0 + 1u);
    asm volatile("s_barrier" ::: "memory");
    Q_R4(x0, w0, 0, 0) Q_R4(x0, w0, 0, 4) Q_R4(x0, w0, 0, 8) Q_R4(x0, w0, 0, 12)
#define Q_GO(CX, CW, NX, NW, i, g, o) Q_MM(CX, CW, g + o); Q_RD(NX, NW, i, g); SBAR();
#define Q_GO4(CX, CW, NX, NW, i, g, o) Q_GO(CX, CW, NX, NW, i, g, o) Q_GO(CX, CW, NX, NW, i, g + 1, o) Q_GO(CX, CW, NX, NW, i, g + 2, o) Q_GO(CX, CW, NX, NW, i, g + 3, o)
#pragma clang loop unroll(disable)
    for (int s = 0; s < 7; ++s) {
      SBAR();
      Q_HEAD(x0, w0, x1, w1, 1)
      cur = (cur == 2 * A_SLOT) ? 0 : cur + A_SLOT;
      Q_MM(x0, w0, 16); Q_AD(0); SBAR(); Q_MM(x0, w0, 17); Q_AD(1); SBAR(); Q_MM(x0, w0, 18); Q_AD(2); SBAR(); Q_MM(x0, w0, 19); Q_AD(3); SBAR();
      Q_M4(x0, w0, 20) Q_M4(x1, w1, 0) Q_M4(x1, w1, 4)
      if (__builtin_expect(wid == 0 && (s == 3 || server), 0)) {
        if (s == 3) DRAIN();
        if (srv && s >= 2) {
          if (s > 2) __builtin_amdgcn_raw_buffer_store_b128(rq, rs, WS_ACK + lane * 16, 0, 16);
          rq = __builtin_amdgcn_raw_buffer_load_b128(rs, WS_REQ + lane * 16, 0, 16);
        }
      }
      LBAR();
      Q_GO4(x1, w1, x0, w0, 0, 0, 8) Q_GO4(x1, w1, x0, w0, 0, 4, 8) Q_GO4(x1, w1, x0, w0, 0, 8, 8) Q_GO4(x1, w1, x0, w0, 0, 12, 8)
    }
    Q_HEAD(x0, w0, x1, w1, 1)
    Q_M4(x0, w0, 16) Q_M4(x0, w0, 20) SBAR();
    Q_M4(x1, w1, 0) Q_M4(x1, w1, 4) Q_M4(x1, w1, 8) Q_M4(x1, w1, 12) Q_M4(x1, w1, 16) Q_M4(x1, w1, 20)
    asm volatile("s_nop 7\n\ts_nop 3" : "+v"(acc[5][0]), "+v"(acc[5][1]), "+v"(acc[5][2]), "+v"(acc[5][3]), "+v"(acc[4][0]), "+v"(acc[4][1]), "+v"(acc[4][2]), "+v"(acc[4][3]));
    asm volatile("" : "+v"(acc[0][0]), "+v"(acc[0][1]), "+v"(acc[0][2]), "+v"(acc[0][3]), "+v"(acc[1][0]), "+v"(acc[1][1]), "+v"(acc[1][2]), "+v"(acc[1][3]));
    asm volatile("" : "+v"(acc[2][0]), "+v"(acc[2][1]), "+v"(acc[2][2]), "+v"(acc[2][3]), "+v"(acc[3][0]), "+v"(acc[3][1]), "+v"(acc[3][2]), "+v"(acc[3][3]));
    __builtin_amdgcn_s_waitcnt(0x0F70);
    if (srv) __builtin_amdgcn_raw_buffer_store_b128(rq, rs, WS_ACK + lane * 16, 0, 16);
    const unsigned b = lds0 + lane * 16 + wid * 4096, go = WS_KV + (unsigned)(jb * 16 + hp) * 32768u + wid * 4096 + lane * 16;
    u32x4 kk[4], vv[4];
#pragma unroll
    for (int t = 0; t < 2; ++t) {
      kk[2 * t] = join16(acc[0][2 * t], acc[0][2 * t + 1]); kk[2 * t + 1] = join16(acc[1][2 * t], acc[1][2 * t + 1]);
      *(AS3 u32x4*)(b + A_K + 16384 + t * 2048) = kk[2 * t]; *(AS3 u32x4*)(b + A_K + 16384 + t * 2048 + 1024) = kk[2 * t + 1];
      vv[2 * t] = natural(join16(acc[2][2 * t], acc[3][2 * t])); vv[2 * t + 1] = natural(join16(acc[2][2 * t + 1], acc[3][2 * t + 1]));
      *(AS3 u32x4*)(b + A_V + 16384 + t * 2048) = vv[2 * t]; *(AS3 u32x4*)(b + A_V + 16384 + t * 2048 + 1024) = vv[2 * t + 1];
      *(AS3 u32x4*)(b + A_Q + t * 2048) = join16(acc[4][2 * t] * QSCALE, acc[4][2 * t + 1] * QSCALE);
      *(AS3 u32x4*)(b + A_Q + t * 2048 + 1024) = join16(acc[5][2 * t] * QSCALE, acc[5][2 * t + 1] * QSCALE);
    }
    WAITL_BAR();
    if (jb < 7) {
#pragma unroll
      for (int i = 0; i < 4; ++i) { __builtin_amdgcn_raw_buffer_store_b128(kk[i], rs, go + i * 1024, 0, 16); __builtin_amdgcn_raw_buffer_store_b128(vv[i], rs, go + 16384 + i * 1024, 0, 16); }
    }
  }
}
DI void a_attn(const Ptrs& p, __amdgpu_buffer_rsrc_t rs, int jb, int hp, int e0, int kq, int flip, int wid, unsigned lds0, bf16x8 (&wof)[8][2]) {
  char* ws = p.ws;
  const int lane = threadIdx.x & 63, h = lane >> 5, r32 = lane & 31;
  const int hd = wid >> 2, qb = wid & 3, qa = 7 - qb;
  const unsigned kb = lds0 + A_K + hd * 1024 + lane * 16;
  const bf16x8 fa = lds128(lds0 + A_Q + qa * 2048 + hd * 1024 + lane * 16), fb = lds128(lds0 + A_Q + qb * 2048 + hd * 1024 + lane * 16);
  const bool real = (r32 >> 4) == hd;
  const unsigned vb = (real ? lds0 + A_V : lds0 + A_ONE) + lane * 16, vs = real ? 2048u : 0u;
  f32x16 oa = {}, ob = {};
  switch (qb) {
    case 0: attention<0>(p, rs, jb, hp, e0, wid, kq, flip, lds0, kb, vb, vs, fa, fb, oa, ob, wof); break;
    case 1: attention<1>(p, rs, jb, hp, e0, wid, kq, flip, lds0, kb, vb, vs, fa, fb, oa, ob, wof); break;
    case 2: attention<2>(p, rs, jb, hp, e0, wid, kq, flip, lds0, kb, vb, vs, fa, fb, oa, ob, wof); break;
    default: attention<3>(p, rs, jb, hp, e0, wid, kq, flip, lds0, kb, vb, vs, fa, fb, oa, ob, wof); break;
  }
  auto put = [&]() {
    const unsigned oo = (unsigned)(jb * 8 * 96 + 64 + hp * 2 + hd) * 1024u + lane * 16;
    u32x4 ra, rb;
    if (hd == 0) { ra = pack8<0>(oa, __builtin_amdgcn_rcpf(oa[8])); rb = pack8<0>(ob, __builtin_amdgcn_rcpf(ob[8])); }
    else { ra = pack8<1>(oa, __builtin_amdgcn_rcpf(oa[0])); rb = pack8<1>(ob, __builtin_amdgcn_rcpf(ob[0])); }
    __builtin_amdgcn_raw_buffer_store_b128(natural(ra), rs, oo + qa * (96 * 1024), 0, 16);
    __builtin_amdgcn_raw_buffer_store_b128(natural(rb), rs, oo + qb * (96 * 1024), 0, 16);
  };
  put();
  asm volatile("" : "+v"(oa), "+v"(ob) :: "memory");
  {
    float t = 0.f;
#pragma unroll
    for (int r = 0; r < 16; ++r) t += __builtin_fabsf(oa[r]) + __builtin_fabsf(ob[r]);
    const float la = hd ? oa[0] : oa[8], lb = hd ? ob[0] : ob[8];
    if (__builtin_expect(__any(!(t < SAFE_HI) || !(la > SAFE_LO) || !(lb > SAFE_LO)), 0)) {
_Pragma("unroll 1")      for (int z = 0; z < 2; ++z) {
        const int q = z ? qb : qa;
        const f32x16 o = att_exact(kb + q * 2048, vb + q * vs, vs, jb == 0 ? 8 - q : 0, z ? fb : fa, h, r32);
        if (z) ob = o; else oa = o;
      }
      DRAIN();
      put();
    }
  }
  if (wid < 4) DRAIN();
  WAITL_BAR();
  if (wid < 4) {
    const char* dg = (const char*)(flip ? p.dr : p.di) + (size_t)e0 * 1024 + wid * 256;
    unsigned vr[4]; piece4<true>(vr, lane);
#pragma unroll
    for (int i = 0; i < 4; ++i) dma4(dg + (i & 1) * 32768 + (i >> 1) * 128, vr, lds0 + wid * 16384 + i * 4096);
  } else {
    DRAIN();
    if (last_of(lds0 + A_CNT + 4, 4, lane)) raise(ws, WS_PF + (128 + jb * 16 + hp) * 128, lane);
  }
}

__global__ __launch_bounds__(512, 2) void fused(Ptrs) {
  extern __shared__ __attribute__((aligned(16))) char smem[];
  const unsigned lds0 = (unsigned)(uintptr_t)smem;
  u64x8 ka; u64x2 kb2;
  asm volatile("s_load_dwordx16 %0, %2, 0x0\n\ts_load_dwordx4 %1, %2, 0x40" : "=&s"(ka), "=&s"(kb2) : "s"(__builtin_amdgcn_kernarg_segment_ptr()));
  unsigned long long pc; asm volatile("s_getpc_b64 %0" : "=s"(pc));
  const int tid = threadIdx.x, lane = tid & 63, wid = __builtin_amdgcn_readfirstlane(tid >> 6);
  const int xcd = blockIdx.x & 7, idx = blockIdx.x >> 3;
  if (wid == 3) {
    const unsigned o = (unsigned)(idx * 16 + (lane & 15)) * 128;
    touch((const void*)(pc & ~127ull), o < CODE_BYTES ? o : 0u, lds0 + A_DUMMY);
  }
  Loader ld;
  ld.init(idx < 16, xcd, idx, wid & 3, lane, lds0);
  asm volatile("s_waitcnt lgkmcnt(0)" : "+s"(ka), "+s"(kb2));
  Ptrs p;
  p.x = (const u16*)ka[0]; p.wqkv = (const u16*)ka[1]; p.cr = (const u16*)ka[2]; p.ci = (const u16*)ka[3]; p.gw = (const u16*)ka[4];
  p.ws = (char*)ka[5]; p.wo = (const u16*)ka[6]; p.dr = (const u16*)ka[7]; p.di = (const u16*)kb2[0]; p.out = (u16*)kb2[1];
  ld.bind(p, idx < 16, idx);
  unsigned ack0 = 0;
  if (wid == 0) ack0 = ldw(p.ws, WS_ACK + blockIdx.x * 4);
  if (idx >= 16 && wid == 7) dma16(p.gw, lane * 16, lds0 + A_GW);
  if (wid >= 4) asm volatile("s_barrier" ::: "memory");
  ld.issue(wid >> 2, idx < 16 ? (wid >> 2) * UNIT : ((wid >> 2) + 1) * A_SLOT, idx >= 16);
  if (wid < 4) asm volatile("s_barrier" ::: "memory");
  char* ws = p.ws;
  const int tp = xcd * 4 + (idx & 3), e0 = (((idx >> 2) & 3) * 2 + (((idx >> 4) + idx) & 1)) * 64;
  const bool server = blockIdx.x == SERVER_BLOCK, isU = idx < 16;
  const int jb = a_jb(xcd, idx), hp = a_hp(xcd, idx);
  const __amdgpu_buffer_rsrc_t rs = __builtin_amdgcn_make_buffer_rsrc(ws, 0, WS_END, 0x00020000);
  const int th = wid & 1, kq = wid >> 1, h = lane >> 5, r32 = lane & 31;
  const int flip = isU ? 0 : (idx >> 1) & 1;
  bf16x8 wof[8][2];
  if (__builtin_expect(wid >= 4, 1)) {
    if (isU) u_loader(ld);
    else a_loader(p, ld, rs, jb, hp, wid, lds0);
    if (isU) u_loader_tail(p, e0, kq, wid, lds0, wof);
  } else {
    if (server && wid == 0) {
#pragma unroll
      for (int i = 0; i < 6; ++i) stw(ws, WS_PF + (i * 64 + lane) * 128, 0u);
    }
    if (isU) u_compute(p, rs, xcd, idx, kq, wid, lds0, ack0, wof);
    else a_compute(p, rs, jb, hp, wid, server, lds0, ack0);
  }
  if (!isU) a_attn(p, rs, jb, hp, e0, kq, flip, wid, lds0, wof);
  const int fi = lane & 7;
  const unsigned pf = WS_PF + (fi < 2 ? ((tp >> 1) * 2 + fi) * 4 + kq : 128 + (tp >> 2) * 16 + (fi < 6 ? kq * 4 + fi - 2 : (tp & 3) * 4 + th * 2 + fi - 6)) * 128;
  auto state = [&](unsigned v) {
    const unsigned m = (unsigned)__ballot(v != 0);
    return ((m & 3u) == 3u ? 1u : 0u) | ((m & 0xFCu) == 0xFCu ? 2u : 0u);
  };
  auto poll = [&](unsigned need) {
    unsigned st = 0;
_Pragma("unroll 1")    for (unsigned spins = 0; spins < SPIN_CAP; ++spins) {
      st = state(ldw(ws, pf));
      if (st & need) break;
      if (server) serve(rs, lane);
      __builtin_amdgcn_s_sleep(12);
    }
    asm volatile("" ::: "memory");
    return st;
  };
  unsigned lp[4];
#pragma unroll
  for (int i = 0; i < 4; ++i) lp[i] = lds0 + (unsigned)(r32 * 128 + (((2 * i + h) ^ ((r32 >> 1) & 7)) * 16) + (kq & 1) * 16384);
  const unsigned br = isU ? ((kq & 2) ? 0 : 4 * UNIT) : ((kq & 2) ? 3 * UNIT : 2 * UNIT), bi = isU ? ((kq & 2) ? 2 * UNIT : UNIT) : ((kq & 2) ? UNIT : 0);
  auto rbase = [&](int d) { return lds0 + (th ? (isU ? ((d & 2) ? 0 : 4 * UNIT) : ((d & 2) ? 3 * UNIT : 2 * UNIT)) + (d & 1) * 16384 : (isU ? 3 * UNIT : 4 * UNIT) + d * 8192); };
  unsigned rw[4], rr;
#pragma unroll
  for (int d = 0; d < 4; ++d) rw[d] = rbase(d) + kq * 2048 + ((lane ^ (d << 2)) * 16);
  rr = rbase((lane >> 1) & 3) + (lane & 1) * 1024 + (((kq * 8 + (lane >> 3)) ^ ((lane & 7) << 1)) * 16);
  unsigned oofs = (unsigned)(((tp * 64 + th * 32 + kq * 8 + (lane >> 3)) * 512 + e0 + (lane & 7) * 8) * 2);
  asm volatile("" : "+v"(rw[0]), "+v"(rw[1]), "+v"(rw[2]), "+v"(rw[3]), "+v"(rr), "+v"(oofs));
  if ((wid >= 4) == isU) DRAIN();
  WAITL_BAR();
  const unsigned v0 = ldw(ws, pf);
  bf16x8 wf[4][2];
#pragma unroll
  for (int i = 0; i < 4; ++i) { wf[i][0] = lds128(lp[i] + br); wf[i][1] = lds128(lp[i] + br + 4096); }
  WAITL_BAR();
  unsigned st = state(v0);
  if (st == 0) { __builtin_amdgcn_s_sleep(6); st = poll(3u); }
  asm volatile("" ::: "memory");
  f32x16 ao[2] = {}, acc[2] = {};
  float gate = 0.f, omg = 0.f;
  {
    const unsigned so = (unsigned)((tp * 2 + th) * 96 + kq * 8) * 1024u;
#define LDACT(seg, i) __builtin_amdgcn_raw_buffer_load_b128(rs, lane * 16 + ((i) & 3) * 1024, so + (seg) * 32768 + ((i) >> 2) * 4096, 16)
#define PIN2(a) asm volatile("" : "+v"(a[0]), "+v"(a[1]) :: "memory")
    const unsigned gofs = WS_G + (tp * 4 + th * 2 + (r32 >> 4)) * 128 + (r32 & 15) * 4;
    auto run = [&](auto yfc) { constexpr bool YF = yfc; constexpr int SW = YF ? 16 : 8;
      u32x4 q[24];
      auto ld = [&](auto jc) { constexpr int j = jc, y = YF ? j : j - 8;
        if constexpr (j == SW) { if (st != 3u) poll(YF ? 2u : 1u); }
        if constexpr (j < 24) {
          if constexpr (y < 0 || y >= 16) { q[j] = LDACT(2, j & 7);
            if constexpr ((j & 7) == 0) { gate = __uint_as_float(__builtin_amdgcn_raw_buffer_load_b32(rs, gofs, 0, 16)); omg = __uint_as_float(__builtin_amdgcn_raw_buffer_load_b32(rs, gofs + 64, 0, 16)); } }
          else q[j] = y < 8 ? LDACT(flip, j & 7) : LDACT(flip ^ 1, j & 7);
        }
        SBAR(); };
      sfor<DEPTH>(ld);
      sfor<24>([&](auto jc) { constexpr int i = jc, y = YF ? i : i - 8;
        const bf16x8 b = asbf(q[i]);
        if constexpr (y >= 0 && y < 16) { acc[0] = MFMA(wf[y & 3][0], b, acc[0]); acc[1] = MFMA(wf[y & 3][1], b, acc[1]); PIN2(acc); }
        else { ao[0] = MFMA(wof[i & 7][0], b, ao[0]); ao[1] = MFMA(wof[i & 7][1], b, ao[1]); PIN2(ao); }
        if constexpr (y >= 0 && y < 12) { const unsigned a = lp[y & 3] + (y < 4 ? br + 8192 : (y < 8 ? bi : bi + 8192)); wf[y & 3][0] = lds128(a); wf[y & 3][1] = lds128(a + 4096); }
        if constexpr (i == (YF ? 20 : 23)) {
#pragma unroll
          for (int r = 0; r < 16; ++r) { acc[0][r] *= omg; acc[1][r] *= omg; }
          PIN2(acc);
        }
        ld(std::integral_constant<int, i + DEPTH>{});
      });
#pragma unroll
      for (int r = 0; r < 16; ++r) { ao[0][r] = __builtin_fmaf(gate, ao[0][r], acc[0][r]); ao[1][r] = __builtin_fmaf(gate, ao[1][r], acc[1][r]); }
    };
    if (st & 1u) run(std::true_type{}); else run(std::false_type{});
  }
  unsigned fl[4];
  auto ldflags = [&]() {
#pragma unroll
    for (int i = 0; i < 4; ++i) fl[i] = ldw(ws, WS_PF + (i * 64 + lane) * 128); };
  if (__builtin_expect(server && wid == 0, 0)) ldflags();
#pragma unroll
  for (int d = 0; d < 4; ++d) {
    const f32x16& a = ao[d >> 1]; const int o = (d & 1) * 8;
    *(AS3 f32x4*)rw[d] = f32x4{a[o], a[o + 1], a[o + 2], a[o + 3]}; *(AS3 f32x4*)((rw[d] ^ 32u) + 1024) = f32x4{a[o + 4], a[o + 5], a[o + 6], a[o + 7]};
  }
  WAITL_BAR();
  {
    f32x4 t[8];
#pragma unroll
    for (int k = 0; k < 8; ++k) t[k] = *(AS3 const f32x4*)(rr + (k >> 1) * 2048 + (k & 1) * 512);
    SBAR();
    const f32x4 s0 = (t[0] + t[2]) + (t[4] + t[6]), s1 = (t[1] + t[3]) + (t[5] + t[7]);
    u32x4 r; r[0] = pk2(s0[0], s0[1]); r[1] = pk2(s0[2], s0[3]); r[2] = pk2(s1[0], s1[1]); r[3] = pk2(s1[2], s1[3]);
    __builtin_amdgcn_raw_buffer_store_b128(r, __builtin_amdgcn_make_buffer_rsrc(p.out, 0, 2048 * 512 * 2, 0x00020000), oofs, 0, 16);
  }
  if (__builtin_expect(server && wid == 0, 0)) {
_Pragma("unroll 1")    for (unsigned spins = 0; spins < SPIN_CAP; ++spins) {
      asm volatile("" : "+v"(fl[0]), "+v"(fl[1]), "+v"(fl[2]), "+v"(fl[3]));
      if (__all((fl[0] & fl[1] & fl[2] & fl[3]) != 0)) break;
      serve(rs, lane);
      __builtin_amdgcn_s_sleep(1);
      ldflags();
    }
  }
}

extern "C" void kernel_launch(void* const* d_in, const int* in_sizes, int n_in, void* d_out, int out_size, void* d_ws, size_t ws_size, hipStream_t stream) {
  Ptrs p;
  p.x = (const u16*)d_in[0]; p.wqkv = (const u16*)d_in[1]; p.wo = (const u16*)d_in[2];
  p.cr = (const u16*)d_in[4]; p.ci = (const u16*)d_in[5]; p.dr = (const u16*)d_in[6]; p.di = (const u16*)d_in[7];
  p.gw = (const u16*)d_in[8]; p.ws = (char*)d_ws; p.out = (u16*)d_out;
  (void)hipFuncSetAttribute((const void*)fused, hipFuncAttributeMaxDynamicSharedMemorySize, LDS_BYTES);
  fused<<<256, 512, LDS_BYTES, stream>>>(p);
}
```
